```python
import torch

def _softmax(x, axis=-1):
    return torch.softmax(x, dim=axis)
def _gelu(x):
    return torch.nn.functional.gelu(x, approximate="tanh")
def _silu(x):
    return torch.nn.functional.silu(x)
def _sigmoid(x):
    return torch.sigmoid(x)
def _layernorm(x, axis=-1, eps=1e-5):
    m = x.mean(dim=axis, keepdim=True)
    v = x.var(dim=axis, keepdim=True, unbiased=False)
    return (x - m) / torch.sqrt(v + eps)
def _scatter_add(tgt, idx, src, axis=0):
    return tgt.clone().index_add_(axis, idx, src)
def _segment_sum(src, seg, n):
    out = torch.zeros((n,) + src.shape[1:], dtype=src.dtype)
    return out.index_add_(0, seg, src)
def _pad(x, pw):
    flat = tuple(v for pair in reversed(pw) for v in pair)
    return torch.nn.functional.pad(x, flat)

kernel_name = 'standalone__gqa_block_cached__sampling'

def setup_inputs():

    _w = (lambda a: (lambda t: t.to(torch.bfloat16) if t.is_floating_point() else t)(torch.as_tensor(a)))
    B, Sq, D = 8, 4, 512
    Hq, Hkv, Sk, Dh = 32, 8, 4096, 128
    assert Hq % Hkv == 0, (Hq, Hkv)
    return {
        "x": _w((lambda *s: torch.randn(*s, dtype=torch.bfloat16) * 0.8)(B, Sq, D)),
        "k_cache": _w((lambda *s: torch.randn(*s, dtype=torch.bfloat16) * 0.8)(B, Hkv, Sk, Dh)),
        "v_cache": _w((lambda *s: torch.randn(*s, dtype=torch.bfloat16) * 0.05)(B, Hkv, Sk, Dh)),
        "wq": _w((lambda *s: torch.randn(*s, dtype=torch.bfloat16) * 0.8)(D, Hq * Dh) * (D ** -0.5)),
        "wo": _w((lambda *s: torch.randn(*s, dtype=torch.bfloat16) * 0.05)(Hq * Dh, D) * ((Hq * Dh) ** -0.5)),
    }

def reference(x, k_cache, v_cache, wq, wo):
    B, Sq, D = x.shape
    Hq, Dh = 32, 128
    Hkv = k_cache.shape[1]
    rep = Hq // Hkv
    q = (lambda x, *d: x.permute(*d))((x @ wq).reshape(B, Sq, Hq, Dh), 0, 2, 1, 3)
    k = (lambda x, n, axis: torch.repeat_interleave(x, n, dim=axis))(k_cache, rep, axis=1)
    v = (lambda x, n, axis: torch.repeat_interleave(x, n, dim=axis))(v_cache, rep, axis=1)
    scores = torch.einsum("bhqd,bhkd->bhqk", q, k) * (Dh ** -0.5)
    p = _softmax(scores, axis=-1)
    o = torch.einsum("bhqk,bhkd->bhqd", p, v)
    return (lambda x, *d: x.permute(*d))(o, 0, 2, 1, 3).reshape(B, Sq, Hq * Dh) @ wo
```

```cpp
#include <hip/hip_runtime.h>
#include <hip/hip_bf16.h>
#include <stdint.h>

typedef __attribute__((ext_vector_type(8))) short bf16x8;
typedef __attribute__((ext_vector_type(4))) float f32x4;
typedef __attribute__((ext_vector_type(4))) unsigned u32x4;

constexpr int B = 8, SQ = 4, DM = 512, HQ = 32, HKV = 8, SK = 4096, DH = 128;
constexpr int NSPLIT = 4, NW = 8, KPW = SK / NSPLIT / NW, TILE = 32, NT = KPW / TILE;
constexpr float L2E = 1.4426950408889634f;
constexpr float SCALE = 0.08838834764831845f;

typedef __attribute__((ext_vector_type(2))) __bf16 bf16x2_t;
typedef __attribute__((ext_vector_type(2))) float f32x2_t;
__device__ __forceinline__ unsigned cvtpk(float lo, float hi) { f32x2_t v = {lo, hi}; bf16x2_t r = __builtin_convertvector(v, bf16x2_t); return __builtin_bit_cast(unsigned, r); }
__device__ __forceinline__ float bf16r(float x) { unsigned u = cvtpk(x, x); return __uint_as_float(u << 16); }
__device__ __forceinline__ u32x4 ldnt(const void* p) { return __builtin_nontemporal_load((const u32x4*)p); }
__device__ __forceinline__ u32x4 ld(const void* p) { return *(const u32x4*)p; }
__device__ __forceinline__ bf16x8 asbf(u32x4 v) { return __builtin_bit_cast(bf16x8, v); }
__device__ __forceinline__ u32x4 tcol(const u32x4* R, int i) {
  const int w = i >> 1; const unsigned sel = (i & 1) ? 0x07060302u : 0x05040100u;
  u32x4 o;
  o[0] = __builtin_amdgcn_perm(R[1][w], R[0][w], sel);
  o[1] = __builtin_amdgcn_perm(R[3][w], R[2][w], sel);
  o[2] = __builtin_amdgcn_perm(R[5][w], R[4][w], sel);
  o[3] = __builtin_amdgcn_perm(R[7][w], R[6][w], sel);
  return o;
}

__global__ __launch_bounds__(512) void qproj_kernel(const uint16_t* __restrict__ x, const uint16_t* __restrict__ wq, uint16_t* __restrict__ q) {
  __shared__ float red[8][32][132];
  const int cc = blockIdx.x;
  const int wave = threadIdx.x >> 6, lane = threadIdx.x & 63, g = lane >> 4, m = lane & 15;
  f32x4 acc[2][8];
#pragma unroll
  for (int rt = 0; rt < 2; ++rt)
#pragma unroll
    for (int i = 0; i < 8; ++i) acc[rt][i] = (f32x4){0.f, 0.f, 0.f, 0.f};
  u32x4 R[2][8], A[2][2];
#pragma unroll
  for (int ks = 0; ks < 2; ++ks) {
    const int kb = wave * 64 + ks * 32;
#pragma unroll
    for (int j = 0; j < 8; ++j) R[ks][j] = ld(wq + (size_t)(kb + 8 * g + j) * 4096 + cc * 128 + 8 * m);
#pragma unroll
    for (int rt = 0; rt < 2; ++rt) A[ks][rt] = ld(x + (rt * 16 + m) * 512 + kb + 8 * g);
  }
#pragma unroll
  for (int ks = 0; ks < 2; ++ks)
#pragma unroll
    for (int i = 0; i < 8; ++i) {
      u32x4 c = tcol(R[ks], i);
#pragma unroll
      for (int rt = 0; rt < 2; ++rt)
        acc[rt][i] = __builtin_amdgcn_mfma_f32_16x16x32_bf16(asbf(A[ks][rt]), asbf(c), acc[rt][i], 0, 0, 0);
    }
#pragma unroll
  for (int rt = 0; rt < 2; ++rt)
#pragma unroll
    for (int r = 0; r < 4; ++r)
#pragma unroll
      for (int i = 0; i < 8; ++i) red[wave][rt * 16 + 4 * g + r][8 * m + i] = acc[rt][i][r];
  __syncthreads();
  const int t = threadIdx.x, row = t >> 4, c0 = (t & 15) * 8;
  float s[8];
#pragma unroll
  for (int e = 0; e < 8; ++e) s[e] = 0.f;
#pragma unroll
  for (int w = 0; w < 8; ++w)
#pragma unroll
    for (int e = 0; e < 8; ++e) s[e] += red[w][row][c0 + e];
  u32x4 o = {cvtpk(s[0], s[1]), cvtpk(s[2], s[3]), cvtpk(s[4], s[5]), cvtpk(s[6], s[7])};
  *(u32x4*)(q + row * 4096 + cc * 128 + c0) = o;
}

__global__ __launch_bounds__(512, 2) void attn_kernel(const uint16_t* __restrict__ q, const uint16_t* __restrict__ kc, const uint16_t* __restrict__ vc,
                                                      float* __restrict__ part_o, float* __restrict__ part_ml) {
  __shared__ float so[NW][16][132];
  __shared__ float sm[NW][16], sl[NW][16];
  const int wg = blockIdx.x, hkv = wg & 7, ii = wg >> 3, b = ii >> 2, s = ii & 3;
  const int wave = threadIdx.x >> 6, lane = threadIdx.x & 63, g = lane >> 4, m = lane & 15;
  const size_t kvbase = ((size_t)(b * HKV + hkv) * SK + s * (SK / NSPLIT) + wave * KPW) * DH;
  const uint16_t* kp = kc + kvbase + m * DH + 8 * g;
  const uint16_t* vp = vc + kvbase + (4 * g) * DH + 8 * m;

  u32x4 kf[2][8], vf[2][8];
  auto issue = [&](int t, int st) {
#pragma unroll
    for (int t2 = 0; t2 < 2; ++t2)
#pragma unroll
      for (int c = 0; c < 4; ++c) kf[st][t2 * 4 + c] = ldnt(kp + (size_t)(t * TILE + 16 * t2) * DH + 32 * c);
#pragma unroll
    for (int j = 0; j < 8; ++j) vf[st][j] = ldnt(vp + (size_t)(t * TILE + 16 * (j >> 2) + (j & 3)) * DH);
  };
  issue(0, 0);
  issue(1, 1);
  u32x4 qf[4];
  {
    const uint16_t* qp = q + (b * 4 + (m & 3)) * 4096 + (hkv * 4 + (m >> 2)) * 128 + 8 * g;
#pragma unroll
    for (int c = 0; c < 4; ++c) qf[c] = ld(qp + 32 * c);
  }
  f32x4 o[8];
#pragma unroll
  for (int i = 0; i < 8; ++i) o[i] = (f32x4){0.f, 0.f, 0.f, 0.f};
  float mrow = -__builtin_inff(), lrow = 0.f;
#pragma unroll
  for (int t = 0; t < NT; ++t) {
    const int st = t & 1;
    f32x4 sc[2];
#pragma unroll
    for (int t2 = 0; t2 < 2; ++t2) {
      sc[t2] = (f32x4){0.f, 0.f, 0.f, 0.f};
#pragma unroll
      for (int c = 0; c < 4; ++c) sc[t2] = __builtin_amdgcn_mfma_f32_16x16x32_bf16(asbf(kf[st][t2 * 4 + c]), asbf(qf[c]), sc[t2], 0, 0, 0);
    }
    float sv[8];
#pragma unroll
    for (int j = 0; j < 8; ++j) sv[j] = bf16r(bf16r(sc[j >> 2][j & 3]) * SCALE);
    float tm = sv[0];
#pragma unroll
    for (int j = 1; j < 8; ++j) tm = fmaxf(tm, sv[j]);
    tm = fmaxf(tm, __shfl_xor(tm, 16));
    tm = fmaxf(tm, __shfl_xor(tm, 32));
    const float mnew = fmaxf(mrow, tm);
    const float alpha = __builtin_amdgcn_exp2f((mrow - mnew) * L2E);
    mrow = mnew;
    float p[8], ps = 0.f;
#pragma unroll
    for (int j = 0; j < 8; ++j) { p[j] = __builtin_amdgcn_exp2f((sv[j] - mnew) * L2E); ps += p[j]; }
    lrow = lrow * alpha + ps;
#pragma unroll
    for (int i = 0; i < 8; ++i) o[i] *= alpha;
    u32x4 pf = {cvtpk(p[0], p[1]), cvtpk(p[2], p[3]), cvtpk(p[4], p[5]), cvtpk(p[6], p[7])};
#pragma unroll
    for (int i = 0; i < 8; ++i) {
      u32x4 vt = tcol(vf[st], i);
      o[i] = __builtin_amdgcn_mfma_f32_16x16x32_bf16(asbf(vt), asbf(pf), o[i], 0, 0, 0);
    }
    if (t + 2 < NT) issue(t + 2, st);
  }
  lrow += __shfl_xor(lrow, 16);
  lrow += __shfl_xor(lrow, 32);
  if (g == 0) { sm[wave][m] = mrow; }
  __syncthreads();
  float M = sm[0][m];
#pragma unroll
  for (int w = 1; w < NW; ++w) M = fmaxf(M, sm[w][m]);
  const float sc2 = __builtin_amdgcn_exp2f((mrow - M) * L2E);
  if (g == 0) sl[wave][m] = lrow * sc2;
#pragma unroll
  for (int r = 0; r < 4; ++r) {
    f32x4 a = {o[0][r] * sc2, o[1][r] * sc2, o[2][r] * sc2, o[3][r] * sc2};
    f32x4 c = {o[4][r] * sc2, o[5][r] * sc2, o[6][r] * sc2, o[7][r] * sc2};
    *(f32x4*)&so[wave][m][32 * g + 8 * r] = a;
    *(f32x4*)&so[wave][m][32 * g + 8 * r + 4] = c;
  }
  __syncthreads();
  const int pidx = (b * HKV + hkv) * NSPLIT + s;
  {
    const int t = threadIdx.x, row = t >> 5, d0 = (t & 31) * 4;
    f32x4 a = {0.f, 0.f, 0.f, 0.f};
#pragma unroll
    for (int w = 0; w < NW; ++w) a += *(const f32x4*)&so[w][row][d0];
    *(f32x4*)(part_o + (size_t)pidx * 2048 + row * 128 + d0) = a;
    if (t < 16) {
      float L = 0.f;
#pragma unroll
      for (int w = 0; w < NW; ++w) L += sl[w][t];
      float Mx = sm[0][t];
#pragma unroll
      for (int w = 1; w < NW; ++w) Mx = fmaxf(Mx, sm[w][t]);
      part_ml[(pidx * 16 + t) * 2] = Mx;
      part_ml[(pidx * 16 + t) * 2 + 1] = L;
    }
  }
}

__global__ __launch_bounds__(256) void outproj_kernel(const float* __restrict__ part_o, const float* __restrict__ part_ml, const uint16_t* __restrict__ wo,
                                                      uint16_t* __restrict__ out) {
  __shared__ float os[4][4096];
  __shared__ float red[256][65];
  const int b = blockIdx.x >> 5, nc = blockIdx.x & 31, t = threadIdx.x;
  for (int hkv = 0; hkv < HKV; ++hkv) {
    for (int e = t; e < 2048; e += 256) {
      const int n = e >> 7, d = e & 127;
      const int p0 = (b * HKV + hkv) * NSPLIT;
      float mm[4], ll[4], M = -__builtin_inff();
#pragma unroll
      for (int s = 0; s < 4; ++s) { mm[s] = part_ml[((p0 + s) * 16 + n) * 2]; ll[s] = part_ml[((p0 + s) * 16 + n) * 2 + 1]; M = fmaxf(M, mm[s]); }
      float L = 0.f, a = 0.f;
#pragma unroll
      for (int s = 0; s < 4; ++s) { const float w = __builtin_amdgcn_exp2f((mm[s] - M) * L2E); L += ll[s] * w; a += w * part_o[(size_t)(p0 + s) * 2048 + e]; }
      os[n & 3][(hkv * 4 + (n >> 2)) * 128 + d] = bf16r(a / L);
    }
  }
  __syncthreads();
  float acc[4][16];
#pragma unroll
  for (int sq = 0; sq < 4; ++sq)
#pragma unroll
    for (int c = 0; c < 16; ++c) acc[sq][c] = 0.f;
  for (int kk = 0; kk < 16; ++kk) {
    const int k = t * 16 + kk;
    u32x4 w0 = ld(wo + (size_t)k * 512 + nc * 16), w1 = ld(wo + (size_t)k * 512 + nc * 16 + 8);
    float wv[16];
#pragma unroll
    for (int u = 0; u < 4; ++u) { wv[2 * u] = __uint_as_float(w0[u] << 16); wv[2 * u + 1] = __uint_as_float(w0[u] & 0xffff0000u); wv[8 + 2 * u] = __uint_as_float(w1[u] << 16); wv[8 + 2 * u + 1] = __uint_as_float(w1[u] & 0xffff0000u); }
#pragma unroll
    for (int sq = 0; sq < 4; ++sq) { const float ov = os[sq][k];
#pragma unroll
      for (int c = 0; c < 16; ++c) acc[sq][c] += ov * wv[c]; }
  }
#pragma unroll
  for (int sq = 0; sq < 4; ++sq)
#pragma unroll
    for (int c = 0; c < 16; ++c) red[t][sq * 16 + c] = acc[sq][c];
  __syncthreads();
  if (t < 64) {
    float a = 0.f;
    for (int u = 0; u < 256; ++u) a += red[u][t];
    const int sq = t >> 4, c = t & 15;
    unsigned pk = cvtpk(a, a);
    out[(b * 4 + sq) * 512 + nc * 16 + c] = (uint16_t)(pk & 0xffff);
  }
}

extern "C" void kernel_launch(void* const* d_in, const int* in_sizes, int n_in, void* d_out, int out_size, void* d_ws, size_t ws_size, hipStream_t stream) {
  const uint16_t* x = (const uint16_t*)d_in[0];
  const uint16_t* kc = (const uint16_t*)d_in[1];
  const uint16_t* vc = (const uint16_t*)d_in[2];
  const uint16_t* wq = (const uint16_t*)d_in[3];
  const uint16_t* wo = (const uint16_t*)d_in[4];
  char* ws = (char*)d_ws;
  uint16_t* q = (uint16_t*)ws;
  float* part_o = (float*)(ws + (1 << 20));
  float* part_ml = (float*)(ws + (4 << 20));
  qproj_kernel<<<32, 512, 0, stream>>>(x, wq, q);
  attn_kernel<<<256, 512, 0, stream>>>(q, kc, vc, part_o, part_ml);
  outproj_kernel<<<256, 256, 0, stream>>>(part_o, part_ml, wo, (uint16_t*)d_out);
}
```

```cpp
#include <hip/hip_runtime.h>
#include <hip/hip_bf16.h>
#include <stdint.h>

typedef __attribute__((ext_vector_type(8))) short bf16x8;
typedef __attribute__((ext_vector_type(4))) float f32x4;
typedef __attribute__((ext_vector_type(4))) unsigned u32x4;
typedef __attribute__((ext_vector_type(2))) unsigned u32x2;
typedef __attribute__((ext_vector_type(2))) __bf16 bf16x2_t;
typedef __attribute__((ext_vector_type(2))) float f32x2_t;
#define GAS __attribute__((address_space(1)))
typedef GAS unsigned gu32;
typedef GAS unsigned long long gu64;
#define RLX_AGENT __ATOMIC_RELAXED, __HIP_MEMORY_SCOPE_AGENT
#define PIN() do { asm volatile("" ::: "memory"); __builtin_amdgcn_sched_barrier(0); } while (0)
#define DRAIN() asm volatile("s_waitcnt vmcnt(0)" ::: "memory")

constexpr int B = 8, SQ = 4, DM = 512, HQ = 32, HKV = 8, SK = 4096, DH = 128;
constexpr int NSPLIT = 4, NW = 8, KPW = SK / NSPLIT / NW, TILE = 32, NT = KPW / TILE;
constexpr int QP = 4;
constexpr float L2E = 1.4426950408889634f;
constexpr float SCALE = 0.08838834764831845f;
constexpr int AUX_SC1 = 16;

constexpr size_t WS_QPART = 0;
constexpr size_t WS_PARTO = 2u << 20;
constexpr size_t WS_PARTML = 4u << 20;
constexpr size_t WS_OUTP = 5u << 20;
constexpr size_t WS_SYNC = 6u << 20;
constexpr int NSYNC = 96;

__device__ __forceinline__ unsigned cvtpk(float lo, float hi) { f32x2_t v = {lo, hi}; bf16x2_t r = __builtin_convertvector(v, bf16x2_t); return __builtin_bit_cast(unsigned, r); }
__device__ __forceinline__ float bf16r(float x) { unsigned u = cvtpk(x, x); return __uint_as_float(u << 16); }
__device__ __forceinline__ u32x4 ldnt(const void* p) { return __builtin_nontemporal_load((const u32x4*)p); }
__device__ __forceinline__ u32x4 ld(const void* p) { return *(const u32x4*)p; }
__device__ __forceinline__ bf16x8 asbf(u32x4 v) { return __builtin_bit_cast(bf16x8, v); }
__device__ __forceinline__ u32x4 tcol(const u32x4* R, int i) {
  const int w = i >> 1; const unsigned sel = (i & 1) ? 0x07060302u : 0x05040100u;
  u32x4 o;
  o[0] = __builtin_amdgcn_perm(R[1][w], R[0][w], sel);
  o[1] = __builtin_amdgcn_perm(R[3][w], R[2][w], sel);
  o[2] = __builtin_amdgcn_perm(R[5][w], R[4][w], sel);
  o[3] = __builtin_amdgcn_perm(R[7][w], R[6][w], sel);
  return o;
}

__global__ __launch_bounds__(256) void qproj_kernel(const uint16_t* __restrict__ x, const uint16_t* __restrict__ wq, float* __restrict__ qpart, unsigned* __restrict__ sync) {
  __shared__ float red[4][32][132];
  const int cc = blockIdx.x & 31, kq = blockIdx.x >> 5;
  const int wave = threadIdx.x >> 6, lane = threadIdx.x & 63, g = lane >> 4, m = lane & 15;
  const int kb = kq * 128 + wave * 32;
  u32x4 R[8], A[2];
#pragma unroll
  for (int j = 0; j < 8; ++j) R[j] = ld(wq + (size_t)(kb + 8 * g + j) * 4096 + cc * 128 + 8 * m);
#pragma unroll
  for (int rt = 0; rt < 2; ++rt) A[rt] = ld(x + (rt * 16 + m) * 512 + kb + 8 * g);
  if (blockIdx.x == 0 && threadIdx.x < NSYNC) sync[threadIdx.x * 64] = 0u;
  f32x4 acc[2][8];
#pragma unroll
  for (int i = 0; i < 8; ++i) {
    u32x4 c = tcol(R, i);
#pragma unroll
    for (int rt = 0; rt < 2; ++rt)
      acc[rt][i] = __builtin_amdgcn_mfma_f32_16x16x32_bf16(asbf(A[rt]), asbf(c), (f32x4){0.f, 0.f, 0.f, 0.f}, 0, 0, 0);
  }
#pragma unroll
  for (int rt = 0; rt < 2; ++rt)
#pragma unroll
    for (int r = 0; r < 4; ++r)
#pragma unroll
      for (int i = 0; i < 8; ++i) red[wave][rt * 16 + 4 * g + r][8 * m + i] = acc[rt][i][r];
  __syncthreads();
  const int t = threadIdx.x, row = t >> 3, c0 = (t & 7) * 16;
  float* dst = qpart + ((size_t)kq * 32 + row) * 4096 + cc * 128 + c0;
#pragma unroll
  for (int e = 0; e < 4; ++e) {
    f32x4 s = *(const f32x4*)&red[0][row][c0 + 4 * e];
#pragma unroll
    for (int w = 1; w < 4; ++w) s += *(const f32x4*)&red[w][row][c0 + 4 * e];
    *(f32x4*)(dst + 4 * e) = s;
  }
}

__global__ __launch_bounds__(512, 2) void attn_kernel(const float* __restrict__ qpart, const uint16_t* __restrict__ kc, const uint16_t* __restrict__ vc,
                                                      const uint16_t* __restrict__ wo, float* part_o, float* part_ml, float* outp, unsigned* sync,
                                                      uint16_t* __restrict__ out) {
  __shared__ float so[NW][16][132];
  __shared__ float sm[NW][16], sl[NW][16];
  __shared__ uint16_t qs[4][520];
  __shared__ int s_last;
  const int wg = blockIdx.x, hkv = wg & 7, ii = wg >> 3, b = ii >> 2, s = ii & 3;
  const int tid = threadIdx.x, wave = tid >> 6, lane = tid & 63, g = lane >> 4, m = lane & 15;
  const size_t kvbase = ((size_t)(b * HKV + hkv) * SK + s * (SK / NSPLIT) + wave * KPW) * DH;
  const uint16_t* kp = kc + kvbase + m * DH + 8 * g;
  const uint16_t* vp = vc + kvbase + (4 * g) * DH + 8 * m;

  f32x4 qa[QP];
  {
    const float* qp = qpart + (size_t)(b * 4 + (tid >> 7)) * 4096 + hkv * 512 + (tid & 127) * 4;
#pragma unroll
    for (int p = 0; p < QP; ++p) qa[p] = *(const f32x4*)(qp + (size_t)p * 32 * 4096);
  }
  PIN();
  u32x4 kf[2][8], vf[2][8];
  auto issue = [&](int t, int st) {
#pragma unroll
    for (int t2 = 0; t2 < 2; ++t2)
#pragma unroll
      for (int c = 0; c < 4; ++c) kf[st][t2 * 4 + c] = ldnt(kp + (size_t)(t * TILE + 16 * t2) * DH + 32 * c);
#pragma unroll
    for (int j = 0; j < 8; ++j) vf[st][j] = ldnt(vp + (size_t)(t * TILE + 16 * (j >> 2) + (j & 3)) * DH);
  };
  issue(0, 0);
  issue(1, 1);
  PIN();
  {
    f32x4 qsum = qa[0];
#pragma unroll
    for (int p = 1; p < QP; ++p) qsum += qa[p];
    u32x2 pk = {cvtpk(qsum[0], qsum[1]), cvtpk(qsum[2], qsum[3])};
    *(u32x2*)&qs[tid >> 7][(tid & 127) * 4] = pk;
  }
  __syncthreads();
  u32x4 qf[4];
#pragma unroll
  for (int c = 0; c < 4; ++c) qf[c] = *(const u32x4*)&qs[m & 3][(m >> 2) * 128 + 32 * c + 8 * g];

  f32x4 o[8];
#pragma unroll
  for (int i = 0; i < 8; ++i) o[i] = (f32x4){0.f, 0.f, 0.f, 0.f};
  float mrow = -__builtin_inff(), lrow = 0.f;
  u32x4 wr[2][8];
#pragma unroll
  for (int t = 0; t < NT; ++t) {
    const int st = t & 1;
    f32x4 sc[2];
#pragma unroll
    for (int t2 = 0; t2 < 2; ++t2) {
      sc[t2] = (f32x4){0.f, 0.f, 0.f, 0.f};
#pragma unroll
      for (int c = 0; c < 4; ++c) sc[t2] = __builtin_amdgcn_mfma_f32_16x16x32_bf16(asbf(kf[st][t2 * 4 + c]), asbf(qf[c]), sc[t2], 0, 0, 0);
    }
    float sv[8];
#pragma unroll
    for (int j = 0; j < 8; ++j) sv[j] = bf16r(bf16r(sc[j >> 2][j & 3]) * SCALE);
    float tm = sv[0];
#pragma unroll
    for (int j = 1; j < 8; ++j) tm = fmaxf(tm, sv[j]);
    tm = fmaxf(tm, __shfl_xor(tm, 16));
    tm = fmaxf(tm, __shfl_xor(tm, 32));
    const float mnew = fmaxf(mrow, tm);
    const float alpha = __builtin_amdgcn_exp2f((mrow - mnew) * L2E);
    mrow = mnew;
    float p[8], ps = 0.f;
#pragma unroll
    for (int j = 0; j < 8; ++j) { p[j] = __builtin_amdgcn_exp2f((sv[j] - mnew) * L2E); ps += p[j]; }
    lrow = lrow * alpha + ps;
#pragma unroll
    for (int i = 0; i < 8; ++i) o[i] *= alpha;
    u32x4 pf = {cvtpk(p[0], p[1]), cvtpk(p[2], p[3]), cvtpk(p[4], p[5]), cvtpk(p[6], p[7])};
#pragma unroll
    for (int i = 0; i < 8; ++i) {
      u32x4 vt = tcol(vf[st], i);
      o[i] = __builtin_amdgcn_mfma_f32_16x16x32_bf16(asbf(vt), asbf(pf), o[i], 0, 0, 0);
    }
    if (t + 2 < NT) { issue(t + 2, st); PIN(); }
    else if (t == NT - 2) {
      const uint16_t* wp = wo + (size_t)(hkv * 512 + wave * 64 + 8 * g) * 512 + s * 128 + 8 * m;
#pragma unroll
      for (int ks = 0; ks < 2; ++ks)
#pragma unroll
        for (int j = 0; j < 8; ++j) wr[ks][j] = ld(wp + (size_t)(ks * 32 + j) * 512);
      PIN();
    }
  }
  lrow += __shfl_xor(lrow, 16);
  lrow += __shfl_xor(lrow, 32);
  if (g == 0) { sm[wave][m] = mrow; }
  __syncthreads();
  float M = sm[0][m];
#pragma unroll
  for (int w = 1; w < NW; ++w) M = fmaxf(M, sm[w][m]);
  const float sc2 = __builtin_amdgcn_exp2f((mrow - M) * L2E);
  if (g == 0) sl[wave][m] = lrow * sc2;
#pragma unroll
  for (int r = 0; r < 4; ++r) {
    f32x4 a = {o[0][r] * sc2, o[1][r] * sc2, o[2][r] * sc2, o[3][r] * sc2};
    f32x4 c = {o[4][r] * sc2, o[5][r] * sc2, o[6][r] * sc2, o[7][r] * sc2};
    *(f32x4*)&so[wave][m][32 * g + 8 * r] = a;
    *(f32x4*)&so[wave][m][32 * g + 8 * r + 4] = c;
  }
  __syncthreads();
  const int p0 = (b * HKV + hkv) * NSPLIT, pidx = p0 + s;
  const int row = tid >> 5, d0 = (tid & 31) * 4;
  const __amdgpu_buffer_rsrc_t rsO = __builtin_amdgcn_make_buffer_rsrc((void*)part_o, 0, 256 * 2048 * 4, 0x00020000);
  f32x4 po[NSPLIT];
  float pm[NSPLIT], pl[NSPLIT];
  {
    f32x4 a = {0.f, 0.f, 0.f, 0.f};
#pragma unroll
    for (int w = 0; w < NW; ++w) a += *(const f32x4*)&so[w][row][d0];
    float L = 0.f;
#pragma unroll
    for (int w = 0; w < NW; ++w) L += sl[w][row];
    float Mx = sm[0][row];
#pragma unroll
    for (int w = 1; w < NW; ++w) Mx = fmaxf(Mx, sm[w][row]);
#pragma unroll
    for (int q = 0; q < NSPLIT; ++q) { po[q] = a; pm[q] = Mx; pl[q] = L; }
    __builtin_amdgcn_raw_buffer_store_b128(__builtin_bit_cast(u32x4, a), rsO, (pidx * 2048 + row * 128 + d0) * 4, 0, AUX_SC1);
    if ((tid & 31) == 0)
      __hip_atomic_store((gu64*)(part_ml + (pidx * 16 + row) * 2), ((unsigned long long)__float_as_uint(L) << 32) | __float_as_uint(Mx), RLX_AGENT);
  }
  DRAIN();
  __syncthreads();
  gu32* cntA = (gu32*)(sync + (b * HKV + hkv) * 64);
  if (tid == 0) {
    __hip_atomic_fetch_add(cntA, 1u, RLX_AGENT);
    for (unsigned spins = 0; spins < (1u << 22); ++spins) {
      if (__hip_atomic_load(cntA, RLX_AGENT) >= (unsigned)NSPLIT) break;
      __builtin_amdgcn_s_sleep(1);
    }
  }
  __syncthreads();
#pragma unroll
  for (int q = 0; q < NSPLIT; ++q) {
    if (q != s) {
      po[q] = __builtin_bit_cast(f32x4, __builtin_amdgcn_raw_buffer_load_b128(rsO, ((p0 + q) * 2048 + row * 128 + d0) * 4, 0, AUX_SC1));
      const unsigned long long ml = __hip_atomic_load((gu64*)(part_ml + ((p0 + q) * 16 + row) * 2), RLX_AGENT);
      pm[q] = __uint_as_float((unsigned)ml); pl[q] = __uint_as_float((unsigned)(ml >> 32));
    }
  }
  {
    float Mx = pm[0];
#pragma unroll
    for (int q = 1; q < NSPLIT; ++q) Mx = fmaxf(Mx, pm[q]);
    float L = 0.f;
    f32x4 a = {0.f, 0.f, 0.f, 0.f};
#pragma unroll
    for (int q = 0; q < NSPLIT; ++q) { const float w = __builtin_amdgcn_exp2f((pm[q] - Mx) * L2E); L += pl[q] * w; a += po[q] * w; }
    const float inv = 1.f / L;
    u32x2 pk = {cvtpk(a[0] * inv, a[1] * inv), cvtpk(a[2] * inv, a[3] * inv)};
    *(u32x2*)&qs[row & 3][(row >> 2) * 128 + d0] = pk;
  }
  __syncthreads();
  {
    f32x4 acc[8];
#pragma unroll
    for (int i = 0; i < 8; ++i) acc[i] = (f32x4){0.f, 0.f, 0.f, 0.f};
#pragma unroll
    for (int ks = 0; ks < 2; ++ks) {
      const u32x4 af = *(const u32x4*)&qs[m & 3][wave * 64 + ks * 32 + 8 * g];
#pragma unroll
      for (int i = 0; i < 8; ++i) {
        u32x4 c = tcol(wr[ks], i);
        acc[i] = __builtin_amdgcn_mfma_f32_16x16x32_bf16(asbf(af), asbf(c), acc[i], 0, 0, 0);
      }
    }
    float* red = &so[0][0][0];
    if (g == 0) {
#pragma unroll
      for (int r = 0; r < 4; ++r) {
        f32x4 a = {acc[0][r], acc[1][r], acc[2][r], acc[3][r]};
        f32x4 c = {acc[4][r], acc[5][r], acc[6][r], acc[7][r]};
        *(f32x4*)&red[(wave * 4 + r) * 132 + 8 * m] = a;
        *(f32x4*)&red[(wave * 4 + r) * 132 + 8 * m + 4] = c;
      }
    }
  }
  __syncthreads();
  const __amdgpu_buffer_rsrc_t rsP = __builtin_amdgcn_make_buffer_rsrc((void*)outp, 0, 256 * 512 * 4, 0x00020000);
  if (tid < 128) {
    const float* red = &so[0][0][0];
    const int r = tid >> 5, c0 = (tid & 31) * 4;
    f32x4 a = *(const f32x4*)&red[r * 132 + c0];
#pragma unroll
    for (int w = 1; w < NW; ++w) a += *(const f32x4*)&red[(w * 4 + r) * 132 + c0];
    __builtin_amdgcn_raw_buffer_store_b128(__builtin_bit_cast(u32x4, a), rsP, (((b * 4 + s) * 8 + hkv) * 512 + r * 128 + c0) * 4, 0, AUX_SC1);
  }
  DRAIN();
  __syncthreads();
  if (tid == 0) {
    gu32* cntC = (gu32*)(sync + (64 + b * 4 + s) * 64);
    const unsigned old = __hip_atomic_fetch_add(cntC, 1u, RLX_AGENT);
    s_last = (old == (unsigned)(HKV - 1));
  }
  __syncthreads();
  if (s_last && tid < 128) {
    const int r = tid >> 5, c0 = (tid & 31) * 4;
    f32x4 a = {0.f, 0.f, 0.f, 0.f};
#pragma unroll
    for (int h = 0; h < HKV; ++h)
      a += __builtin_bit_cast(f32x4, __builtin_amdgcn_raw_buffer_load_b128(rsP, (((b * 4 + s) * 8 + h) * 512 + r * 128 + c0) * 4, 0, AUX_SC1));
    u32x2 pk = {cvtpk(a[0], a[1]), cvtpk(a[2], a[3])};
    *(u32x2*)(out + (b * 4 + r) * 512 + s * 128 + c0) = pk;
  }
}

extern "C" void kernel_launch(void* const* d_in, const int* in_sizes, int n_in, void* d_out, int out_size, void* d_ws, size_t ws_size, hipStream_t stream) {
  const uint16_t* x = (const uint16_t*)d_in[0];
  const uint16_t* kc = (const uint16_t*)d_in[1];
  const uint16_t* vc = (const uint16_t*)d_in[2];
  const uint16_t* wq = (const uint16_t*)d_in[3];
  const uint16_t* wo = (const uint16_t*)d_in[4];
  char* ws = (char*)d_ws;
  float* qpart = (float*)(ws + WS_QPART);
  float* part_o = (float*)(ws + WS_PARTO);
  float* part_ml = (float*)(ws + WS_PARTML);
  float* outp = (float*)(ws + WS_OUTP);
  unsigned* sync = (unsigned*)(ws + WS_SYNC);
  qproj_kernel<<<128, 256, 0, stream>>>(x, wq, qpart, sync);
  attn_kernel<<<256, 512, 0, stream>>>(qpart, kc, vc, wo, part_o, part_ml, outp, sync, (uint16_t*)d_out);
}
```

```cpp
#include <hip/hip_runtime.h>
#include <hip/hip_bf16.h>
#include <stdint.h>

typedef __attribute__((ext_vector_type(8))) short bf16x8;
typedef __attribute__((ext_vector_type(4))) float f32x4;
typedef __attribute__((ext_vector_type(4))) unsigned u32x4;
typedef __attribute__((ext_vector_type(2))) unsigned u32x2;
typedef __attribute__((ext_vector_type(2))) __bf16 bf16x2_t;
typedef __attribute__((ext_vector_type(2))) float f32x2_t;
#define GAS __attribute__((address_space(1)))
typedef GAS unsigned gu32;
typedef GAS unsigned long long gu64;
#define RLX_AGENT __ATOMIC_RELAXED, __HIP_MEMORY_SCOPE_AGENT
#define PIN() do { asm volatile("" ::: "memory"); __builtin_amdgcn_sched_barrier(0); } while (0)
#define DRAIN() asm volatile("s_waitcnt vmcnt(0)" ::: "memory")
#define STAMP(k) do {} while (0)
#define DBGARG
#define DBGPASS

constexpr int B = 8, SQ = 4, DM = 512, HQ = 32, HKV = 8, SK = 4096, DH = 128;
constexpr int NSPLIT = 4, NW = 8, KPW = SK / NSPLIT / NW, TILE = 32, NT = KPW / TILE;
constexpr int QP = 4;
constexpr float L2E = 1.4426950408889634f;
constexpr float SCALE = 0.08838834764831845f;
constexpr int AUX_SC1 = 16;

constexpr size_t WS_QPART = 0;
constexpr size_t WS_PARTO = 2u << 20;
constexpr size_t WS_OUTP = 4u << 20;
constexpr size_t WS_PARTML = 5u << 20;
constexpr size_t WS_SYNC = WS_PARTML + (32u << 10);
constexpr int ZERO_BYTES = (1 << 20) + (32 << 10) + 64 * 64;
#define PF_LOADS 0

__device__ __forceinline__ unsigned cvtpk(float lo, float hi) { f32x2_t v = {lo, hi}; bf16x2_t r = __builtin_convertvector(v, bf16x2_t); return __builtin_bit_cast(unsigned, r); }
__device__ __forceinline__ float bf16r(float x) { unsigned u = cvtpk(x, x); return __uint_as_float(u << 16); }
__device__ __forceinline__ u32x4 ldnt(const void* p) { return __builtin_nontemporal_load((const u32x4*)p); }
__device__ __forceinline__ u32x4 ld(const void* p) { return *(const u32x4*)p; }
__device__ __forceinline__ bf16x8 asbf(u32x4 v) { return __builtin_bit_cast(bf16x8, v); }
__device__ __forceinline__ u32x4 tcol(const u32x4* R, int i) {
  const int w = i >> 1; const unsigned sel = (i & 1) ? 0x07060302u : 0x05040100u;
  u32x4 o;
  o[0] = __builtin_amdgcn_perm(R[1][w], R[0][w], sel);
  o[1] = __builtin_amdgcn_perm(R[3][w], R[2][w], sel);
  o[2] = __builtin_amdgcn_perm(R[5][w], R[4][w], sel);
  o[3] = __builtin_amdgcn_perm(R[7][w], R[6][w], sel);
  return o;
}

__global__ __launch_bounds__(256) void qproj_kernel(const uint16_t* __restrict__ x, const uint16_t* __restrict__ wq, float* __restrict__ qpart, u32x4* __restrict__ zero, const uint16_t* __restrict__ kc DBGARG) {
  __shared__ float red[4][32][132];
  if (blockIdx.x >= 128) {
    const int p = blockIdx.x - 128;
    for (int i = p * 256 + threadIdx.x; i < ZERO_BYTES / 16; i += 128 * 256) zero[i] = (u32x4){0u, 0u, 0u, 0u};
    return;
  }
  const int cc = blockIdx.x & 31, kq = blockIdx.x >> 5;
  const int wave = threadIdx.x >> 6, lane = threadIdx.x & 63, g = lane >> 4, m = lane & 15;
  const int kb = kq * 128 + wave * 32;
  STAMP(5);
  u32x4 R[8], A[2];
#pragma unroll
  for (int j = 0; j < 8; ++j) R[j] = ld(wq + (size_t)(kb + 8 * g + j) * 4096 + cc * 128 + 8 * m);
#pragma unroll
  for (int rt = 0; rt < 2; ++rt) A[rt] = ld(x + (rt * 16 + m) * 512 + kb + 8 * g);
  f32x4 acc[2][8];
#pragma unroll
  for (int i = 0; i < 8; ++i) {
    u32x4 c = tcol(R, i);
#pragma unroll
    for (int rt = 0; rt < 2; ++rt)
      acc[rt][i] = __builtin_amdgcn_mfma_f32_16x16x32_bf16(asbf(A[rt]), asbf(c), (f32x4){0.f, 0.f, 0.f, 0.f}, 0, 0, 0);
  }
#pragma unroll
  for (int rt = 0; rt < 2; ++rt)
#pragma unroll
    for (int r = 0; r < 4; ++r)
#pragma unroll
      for (int i = 0; i < 8; ++i) red[wave][rt * 16 + 4 * g + r][8 * m + i] = acc[rt][i][r];
  __syncthreads();
  const int t = threadIdx.x, row = t >> 3, c0 = (t & 7) * 16;
  float* dst = qpart + ((size_t)kq * 32 + row) * 4096 + cc * 128 + c0;
#pragma unroll
  for (int e = 0; e < 4; ++e) {
    f32x4 s = *(const f32x4*)&red[0][row][c0 + 4 * e];
#pragma unroll
    for (int w = 1; w < 4; ++w) s += *(const f32x4*)&red[w][row][c0 + 4 * e];
    *(f32x4*)(dst + 4 * e) = s;
  }
  DRAIN();
  STAMP(6);
}

__global__ __launch_bounds__(512, 2) void attn_kernel(const float* __restrict__ qpart, const uint16_t* __restrict__ kc, const uint16_t* __restrict__ vc,
                                                      const uint16_t* __restrict__ wo, float* part_o, float* part_ml, float* outp, unsigned* sync,
                                                      uint16_t* __restrict__ out DBGARG) {
  __shared__ float so[NW][16][132];
  __shared__ float sm[NW][16], sl[NW][16];
  __shared__ uint16_t qs[4][520];
  const int wg = blockIdx.x, hkv = wg & 7, ii = wg >> 3, b = ii >> 2, s = ii & 3;
  const int tid = threadIdx.x, wave = tid >> 6, lane = tid & 63, g = lane >> 4, m = lane & 15;
  const size_t kvbase = ((size_t)(b * HKV + hkv) * SK + s * (SK / NSPLIT) + wave * KPW) * DH;
  const uint16_t* kp = kc + kvbase + m * DH + 8 * g;
  const uint16_t* vp = vc + kvbase + (4 * g) * DH + 8 * m;

  STAMP(0);
  f32x4 qa[QP];
  {
    const float* qp = qpart + (size_t)(b * 4 + (tid >> 7)) * 4096 + hkv * 512 + (tid & 127) * 4;
#pragma unroll
    for (int p = 0; p < QP; ++p) qa[p] = *(const f32x4*)(qp + (size_t)p * 32 * 4096);
  }
  PIN();
  __builtin_amdgcn_s_barrier();
  PIN();
  u32x4 kf[2][8], vf[2][8];
  auto issue = [&](int t, int st) {
#pragma unroll
    for (int t2 = 0; t2 < 2; ++t2)
#pragma unroll
      for (int c = 0; c < 4; ++c) kf[st][t2 * 4 + c] = ldnt(kp + (size_t)(t * TILE + 16 * t2) * DH + 32 * c);
#pragma unroll
    for (int j = 0; j < 8; ++j) vf[st][j] = ldnt(vp + (size_t)(t * TILE + 16 * (j >> 2) + (j & 3)) * DH);
  };
  issue(0, 0);
  issue(1, 1);
  PIN();
  {
    f32x4 qsum = qa[0];
#pragma unroll
    for (int p = 1; p < QP; ++p) qsum += qa[p];
    u32x2 pk = {cvtpk(qsum[0], qsum[1]), cvtpk(qsum[2], qsum[3])};
    *(u32x2*)&qs[tid >> 7][(tid & 127) * 4] = pk;
  }
  __syncthreads();
  u32x4 qf[4];
#pragma unroll
  for (int c = 0; c < 4; ++c) qf[c] = *(const u32x4*)&qs[m & 3][(m >> 2) * 128 + 32 * c + 8 * g];

  STAMP(1);
  f32x4 o[8];
#pragma unroll
  for (int i = 0; i < 8; ++i) o[i] = (f32x4){0.f, 0.f, 0.f, 0.f};
  float mrow = -__builtin_inff(), lrow = 0.f;
  u32x4 wr[2][8];
#pragma unroll
  for (int t = 0; t < NT; ++t) {
    const int st = t & 1;
    f32x4 sc[2];
#pragma unroll
    for (int t2 = 0; t2 < 2; ++t2) {
      sc[t2] = (f32x4){0.f, 0.f, 0.f, 0.f};
#pragma unroll
      for (int c = 0; c < 4; ++c) sc[t2] = __builtin_amdgcn_mfma_f32_16x16x32_bf16(asbf(kf[st][t2 * 4 + c]), asbf(qf[c]), sc[t2], 0, 0, 0);
    }
    float sv[8];
#pragma unroll
    for (int j = 0; j < 8; ++j) sv[j] = bf16r(bf16r(sc[j >> 2][j & 3]) * SCALE);
    float tm = sv[0];
#pragma unroll
    for (int j = 1; j < 8; ++j) tm = fmaxf(tm, sv[j]);
    tm = fmaxf(tm, __shfl_xor(tm, 16));
    tm = fmaxf(tm, __shfl_xor(tm, 32));
    const float mnew = fmaxf(mrow, tm);
    const float alpha = __builtin_amdgcn_exp2f((mrow - mnew) * L2E);
    mrow = mnew;
    float p[8], ps = 0.f;
#pragma unroll
    for (int j = 0; j < 8; ++j) { p[j] = __builtin_amdgcn_exp2f((sv[j] - mnew) * L2E); ps += p[j]; }
    lrow = lrow * alpha + ps;
#pragma unroll
    for (int i = 0; i < 8; ++i) o[i] *= alpha;
    u32x4 pf = {cvtpk(p[0], p[1]), cvtpk(p[2], p[3]), cvtpk(p[4], p[5]), cvtpk(p[6], p[7])};
#pragma unroll
    for (int i = 0; i < 8; ++i) {
      u32x4 vt = tcol(vf[st], i);
      o[i] = __builtin_amdgcn_mfma_f32_16x16x32_bf16(asbf(vt), asbf(pf), o[i], 0, 0, 0);
    }
    if (t + 2 < NT) { issue(t + 2, st); PIN(); }
    else if (t == NT - 2) {
      const uint16_t* wp = wo + (size_t)(hkv * 512 + wave * 64 + 8 * g) * 512 + s * 128 + 8 * m;
#pragma unroll
      for (int ks = 0; ks < 2; ++ks)
#pragma unroll
        for (int j = 0; j < 8; ++j) wr[ks][j] = ld(wp + (size_t)(ks * 32 + j) * 512);
      PIN();
    }
  }
  STAMP(2);
  lrow += __shfl_xor(lrow, 16);
  lrow += __shfl_xor(lrow, 32);
  if (g == 0) { sm[wave][m] = mrow; sl[wave][m] = lrow; }
#pragma unroll
  for (int r = 0; r < 4; ++r) {
    f32x4 a = {o[0][r], o[1][r], o[2][r], o[3][r]};
    f32x4 c = {o[4][r], o[5][r], o[6][r], o[7][r]};
    *(f32x4*)&so[wave][m][32 * g + 8 * r] = a;
    *(f32x4*)&so[wave][m][32 * g + 8 * r + 4] = c;
  }
  __syncthreads();
  const int p0 = (b * HKV + hkv) * NSPLIT, pidx = p0 + s;
  const int row = tid >> 5, d0 = (tid & 31) * 4;
  const __amdgpu_buffer_rsrc_t rsO = __builtin_amdgcn_make_buffer_rsrc((void*)part_o, 0, 256 * 2048 * 4, 0x00020000);
  f32x4 po[NSPLIT];
  float pm[NSPLIT], pl[NSPLIT];
  {
    float Mx = sm[0][row];
#pragma unroll
    for (int w = 1; w < NW; ++w) Mx = fmaxf(Mx, sm[w][row]);
    f32x4 a = {0.f, 0.f, 0.f, 0.f};
    float L = 0.f;
#pragma unroll
    for (int w = 0; w < NW; ++w) { const float ww = __builtin_amdgcn_exp2f((sm[w][row] - Mx) * L2E); a += *(const f32x4*)&so[w][row][d0] * ww; L += sl[w][row] * ww; }
#pragma unroll
    for (int q = 0; q < NSPLIT; ++q) { po[q] = a; pm[q] = Mx; pl[q] = L; }
    __builtin_amdgcn_raw_buffer_store_b128(__builtin_bit_cast(u32x4, a), rsO, (pidx * 2048 + row * 128 + d0) * 4, 0, AUX_SC1);
    if ((tid & 31) == 0)
      __hip_atomic_store((gu64*)(part_ml + (pidx * 16 + row) * 2), ((unsigned long long)__float_as_uint(L) << 32) | __float_as_uint(Mx), RLX_AGENT);
  }
  DRAIN();
  __syncthreads();
  gu32* flagA = (gu32*)(sync + (b * HKV + hkv) * 16);
  if (tid == 0) {
    __hip_atomic_store(flagA + s, 1u, RLX_AGENT);
    const __amdgpu_buffer_rsrc_t rsF = __builtin_amdgcn_make_buffer_rsrc((void*)sync, 0, 64 * 64, 0x00020000);
    for (unsigned spins = 0; spins < (1u << 22); ++spins) {
      const u32x4 f = __builtin_amdgcn_raw_buffer_load_b128(rsF, (b * HKV + hkv) * 64, 0, AUX_SC1);
      if ((f[0] & f[1] & f[2] & f[3]) == 1u) break;
      __builtin_amdgcn_s_sleep(1);
    }
  }
  __syncthreads();
  STAMP(3);
#pragma unroll
  for (int q = 0; q < NSPLIT; ++q) {
    if (q != s) {
      po[q] = __builtin_bit_cast(f32x4, __builtin_amdgcn_raw_buffer_load_b128(rsO, ((p0 + q) * 2048 + row * 128 + d0) * 4, 0, AUX_SC1));
      const unsigned long long ml = __hip_atomic_load((gu64*)(part_ml + ((p0 + q) * 16 + row) * 2), RLX_AGENT);
      pm[q] = __uint_as_float((unsigned)ml); pl[q] = __uint_as_float((unsigned)(ml >> 32));
    }
  }
  {
    float Mx = pm[0];
#pragma unroll
    for (int q = 1; q < NSPLIT; ++q) Mx = fmaxf(Mx, pm[q]);
    float L = 0.f;
    f32x4 a = {0.f, 0.f, 0.f, 0.f};
#pragma unroll
    for (int q = 0; q < NSPLIT; ++q) { const float w = __builtin_amdgcn_exp2f((pm[q] - Mx) * L2E); L += pl[q] * w; a += po[q] * w; }
    const float inv = 1.f / L;
    u32x2 pk = {cvtpk(a[0] * inv, a[1] * inv), cvtpk(a[2] * inv, a[3] * inv)};
    *(u32x2*)&qs[row & 3][(row >> 2) * 128 + d0] = pk;
  }
  __syncthreads();
  {
    f32x4 acc[8];
#pragma unroll
    for (int i = 0; i < 8; ++i) acc[i] = (f32x4){0.f, 0.f, 0.f, 0.f};
#pragma unroll
    for (int ks = 0; ks < 2; ++ks) {
      const u32x4 af = *(const u32x4*)&qs[m & 3][wave * 64 + ks * 32 + 8 * g];
#pragma unroll
      for (int i = 0; i < 8; ++i) {
        u32x4 c = tcol(wr[ks], i);
        acc[i] = __builtin_amdgcn_mfma_f32_16x16x32_bf16(asbf(af), asbf(c), acc[i], 0, 0, 0);
      }
    }
    float* red = &so[0][0][0];
    if (g == 0) {
#pragma unroll
      for (int r = 0; r < 4; ++r) {
        f32x4 a = {acc[0][r], acc[1][r], acc[2][r], acc[3][r]};
        f32x4 c = {acc[4][r], acc[5][r], acc[6][r], acc[7][r]};
        *(f32x4*)&red[(wave * 4 + r) * 132 + 8 * m] = a;
        *(f32x4*)&red[(wave * 4 + r) * 132 + 8 * m + 4] = c;
      }
    }
  }
  __syncthreads();
  gu64* gran = (gu64*)outp + (size_t)(b * 4 + s) * 8 * 512 + tid;
  {
    const float* red = &so[0][0][0];
    const int r = tid >> 7, c = tid & 127;
    float a = red[r * 132 + c];
#pragma unroll
    for (int w = 1; w < NW; ++w) a += red[(w * 4 + r) * 132 + c];
    __hip_atomic_store(gran + hkv * 512, (1ull << 32) | __float_as_uint(a), RLX_AGENT);
  }
  if (hkv == 0) {
    float v[HKV];
    for (unsigned spins = 0; spins < (1u << 20); ++spins) {
      bool ok = true;
#pragma unroll
      for (int h = 0; h < HKV; ++h) { const unsigned long long x = __hip_atomic_load(gran + h * 512, RLX_AGENT); v[h] = __uint_as_float((unsigned)x); ok &= (x >> 32) == 1ull; }
      if (__all(ok)) break;
      __builtin_amdgcn_s_sleep(1);
    }
    float a = v[0];
#pragma unroll
    for (int h = 1; h < HKV; ++h) a += v[h];
    const unsigned pk = cvtpk(a, a);
    out[(b * 4 + (tid >> 7)) * 512 + s * 128 + (tid & 127)] = (uint16_t)(pk & 0xffffu);
  }
  DRAIN();
  STAMP(4);
}

extern "C" void kernel_launch(void* const* d_in, const int* in_sizes, int n_in, void* d_out, int out_size, void* d_ws, size_t ws_size, hipStream_t stream) {
  const uint16_t* x = (const uint16_t*)d_in[0];
  const uint16_t* kc = (const uint16_t*)d_in[1];
  const uint16_t* vc = (const uint16_t*)d_in[2];
  const uint16_t* wq = (const uint16_t*)d_in[3];
  const uint16_t* wo = (const uint16_t*)d_in[4];
  char* ws = (char*)d_ws;
  float* qpart = (float*)(ws + WS_QPART);
  float* part_o = (float*)(ws + WS_PARTO);
  float* part_ml = (float*)(ws + WS_PARTML);
  float* outp = (float*)(ws + WS_OUTP);
  unsigned* sync = (unsigned*)(ws + WS_SYNC);
  qproj_kernel<<<256, 256, 0, stream>>>(x, wq, qpart, (u32x4*)(ws + WS_OUTP), kc DBGPASS);
  attn_kernel<<<256, 512, 0, stream>>>(qpart, kc, vc, wo, part_o, part_ml, outp, sync, (uint16_t*)d_out DBGPASS);
}
```

```cpp
#include <hip/hip_runtime.h>
#include <hip/hip_bf16.h>
#include <stdint.h>

typedef __attribute__((ext_vector_type(8))) short bf16x8;
typedef __attribute__((ext_vector_type(4))) short bf16x4;
typedef __attribute__((ext_vector_type(4))) float f32x4;
typedef __attribute__((ext_vector_type(4))) unsigned u32x4;
typedef __attribute__((ext_vector_type(2))) unsigned u32x2;
typedef __attribute__((ext_vector_type(2))) __bf16 bf16x2_t;
typedef __attribute__((ext_vector_type(2))) float f32x2_t;
#define GAS __attribute__((address_space(1)))
typedef GAS unsigned gu32;
typedef GAS unsigned long long gu64;
#define RLX_AGENT __ATOMIC_RELAXED, __HIP_MEMORY_SCOPE_AGENT
#define PIN() do { asm volatile("" ::: "memory"); __builtin_amdgcn_sched_barrier(0); } while (0)
#define DRAIN() asm volatile("s_waitcnt vmcnt(0)" ::: "memory")
#define STAMP(k) do {} while (0)
#define DBGARG
#define DBGPASS

constexpr int B = 8, SQ = 4, DM = 512, HQ = 32, HKV = 8, SK = 4096, DH = 128;
constexpr int NSPLIT = 4, NW = 8, KPW = SK / NSPLIT / NW, TILE = 16, NT = KPW / TILE;
#define DEPTH 2
constexpr int QP = 4;
constexpr float L2E = 1.4426950408889634f;
constexpr float SCALE = 0.08838834764831845f;
constexpr int AUX_SC1 = 16;

constexpr size_t WS_QPART = 0;
constexpr size_t WS_PARTO = 2u << 20;
constexpr size_t WS_OUTP = 4u << 20;
constexpr size_t WS_PARTML = 5u << 20;
constexpr size_t WS_SYNC = WS_PARTML + (32u << 10);
constexpr int ZERO_BYTES = (1 << 20) + (32 << 10) + 64 * 64;
#define PF_LOADS 0

__device__ __forceinline__ unsigned cvtpk(float lo, float hi) { f32x2_t v = {lo, hi}; bf16x2_t r = __builtin_convertvector(v, bf16x2_t); return __builtin_bit_cast(unsigned, r); }
__device__ __forceinline__ float bf16r(float x) { unsigned u = cvtpk(x, x); return __uint_as_float(u << 16); }
__device__ __forceinline__ u32x4 ldnt(const void* p) { return __builtin_nontemporal_load((const u32x4*)p); }
__device__ __forceinline__ u32x4 ld(const void* p) { return *(const u32x4*)p; }
__device__ __forceinline__ bf16x8 asbf(u32x4 v) { return __builtin_bit_cast(bf16x8, v); }
__device__ __forceinline__ u32x4 tcol(const u32x4* R, int i) {
  const int w = i >> 1; const unsigned sel = (i & 1) ? 0x07060302u : 0x05040100u;
  u32x4 o;
  o[0] = __builtin_amdgcn_perm(R[1][w], R[0][w], sel);
  o[1] = __builtin_amdgcn_perm(R[3][w], R[2][w], sel);
  o[2] = __builtin_amdgcn_perm(R[5][w], R[4][w], sel);
  o[3] = __builtin_amdgcn_perm(R[7][w], R[6][w], sel);
  return o;
}

__global__ __launch_bounds__(256) void qproj_kernel(const uint16_t* __restrict__ x, const uint16_t* __restrict__ wq, float* __restrict__ qpart, u32x4* __restrict__ zero, const uint16_t* __restrict__ kc DBGARG) {
  __shared__ float red[4][32][132];
  if (blockIdx.x >= 128) {
    const int p = blockIdx.x - 128;
    for (int i = p * 256 + threadIdx.x; i < ZERO_BYTES / 16; i += 128 * 256) zero[i] = (u32x4){0u, 0u, 0u, 0u};
    return;
  }
  const int cc = blockIdx.x & 31, kq = blockIdx.x >> 5;
  const int wave = threadIdx.x >> 6, lane = threadIdx.x & 63, g = lane >> 4, m = lane & 15;
  const int kb = kq * 128 + wave * 32;
  STAMP(5);
  u32x4 R[8], A[2];
#pragma unroll
  for (int j = 0; j < 8; ++j) R[j] = ld(wq + (size_t)(kb + 8 * g + j) * 4096 + cc * 128 + 8 * m);
#pragma unroll
  for (int rt = 0; rt < 2; ++rt) A[rt] = ld(x + (rt * 16 + m) * 512 + kb + 8 * g);
  f32x4 acc[2][8];
#pragma unroll
  for (int i = 0; i < 8; ++i) {
    u32x4 c = tcol(R, i);
#pragma unroll
    for (int rt = 0; rt < 2; ++rt)
      acc[rt][i] = __builtin_amdgcn_mfma_f32_16x16x32_bf16(asbf(A[rt]), asbf(c), (f32x4){0.f, 0.f, 0.f, 0.f}, 0, 0, 0);
  }
#pragma unroll
  for (int rt = 0; rt < 2; ++rt)
#pragma unroll
    for (int r = 0; r < 4; ++r)
#pragma unroll
      for (int i = 0; i < 8; ++i) red[wave][rt * 16 + 4 * g + r][8 * m + i] = acc[rt][i][r];
  __syncthreads();
  const int t = threadIdx.x, row = t >> 3, c0 = (t & 7) * 16;
  float* dst = qpart + ((size_t)kq * 32 + row) * 4096 + cc * 128 + c0;
#pragma unroll
  for (int e = 0; e < 4; ++e) {
    f32x4 s = *(const f32x4*)&red[0][row][c0 + 4 * e];
#pragma unroll
    for (int w = 1; w < 4; ++w) s += *(const f32x4*)&red[w][row][c0 + 4 * e];
    *(f32x4*)(dst + 4 * e) = s;
  }
  DRAIN();
  STAMP(6);
}

__global__ __launch_bounds__(512, 2) void attn_kernel(const float* __restrict__ qpart, const uint16_t* __restrict__ kc, const uint16_t* __restrict__ vc,
                                                      const uint16_t* __restrict__ wo, float* part_o, float* part_ml, float* outp, unsigned* sync,
                                                      uint16_t* __restrict__ out DBGARG) {
  __shared__ float so[NW][16][132];
  __shared__ float sm[NW][16], sl[NW][16];
  __shared__ uint16_t qs[4][520];
  const int wg = blockIdx.x, hkv = wg & 7, ii = wg >> 3, b = ii >> 2, s = ii & 3;
  const int tid = threadIdx.x, wave = tid >> 6, lane = tid & 63, g = lane >> 4, m = lane & 15;
  const size_t kvbase = ((size_t)(b * HKV + hkv) * SK + s * (SK / NSPLIT) + wave * TILE) * DH;
  const uint16_t* kp = kc + kvbase + m * DH + 8 * g;
  const uint16_t* vp = vc + kvbase + (4 * g) * DH + 8 * m;

  STAMP(0);
  f32x4 qa[QP];
  {
    const float* qp = qpart + (size_t)(b * 4 + (tid >> 7)) * 4096 + hkv * 512 + (tid & 127) * 4;
#pragma unroll
    for (int p = 0; p < QP; ++p) qa[p] = *(const f32x4*)(qp + (size_t)p * 32 * 4096);
  }
  PIN();
  __builtin_amdgcn_s_barrier();
  PIN();
  u32x4 kf[DEPTH][4], vf[DEPTH][4];
  auto issue = [&](int t, int st) {
#pragma unroll
    for (int c = 0; c < 4; ++c) kf[st][c] = ldnt(kp + (size_t)(t * NW * TILE) * DH + 32 * c);
#pragma unroll
    for (int j = 0; j < 4; ++j) vf[st][j] = ldnt(vp + (size_t)(t * NW * TILE + j) * DH);
  };
#pragma unroll
  for (int d = 0; d < DEPTH; ++d) issue(d, d);
  PIN();
  {
    f32x4 qsum = qa[0];
#pragma unroll
    for (int p = 1; p < QP; ++p) qsum += qa[p];
    u32x2 pk = {cvtpk(qsum[0], qsum[1]), cvtpk(qsum[2], qsum[3])};
    *(u32x2*)&qs[tid >> 7][(tid & 127) * 4] = pk;
  }
  __syncthreads();
  u32x4 qf[4];
#pragma unroll
  for (int c = 0; c < 4; ++c) qf[c] = *(const u32x4*)&qs[m & 3][(m >> 2) * 128 + 32 * c + 8 * g];

  STAMP(1);
  f32x4 o[8];
#pragma unroll
  for (int i = 0; i < 8; ++i) o[i] = (f32x4){0.f, 0.f, 0.f, 0.f};
  float mrow = -__builtin_inff(), lrow = 0.f;
  u32x4 wr[2][8];
#pragma unroll
  for (int t = 0; t < NT; ++t) {
    const int st = t % DEPTH;
    f32x4 sc = {0.f, 0.f, 0.f, 0.f};
#pragma unroll
    for (int c = 0; c < 4; ++c) sc = __builtin_amdgcn_mfma_f32_16x16x32_bf16(asbf(kf[st][c]), asbf(qf[c]), sc, 0, 0, 0);
    float sv[4];
#pragma unroll
    for (int j = 0; j < 4; ++j) sv[j] = bf16r(bf16r(sc[j]) * SCALE);
    float tm = fmaxf(fmaxf(sv[0], sv[1]), fmaxf(sv[2], sv[3]));
    tm = fmaxf(tm, __shfl_xor(tm, 16));
    tm = fmaxf(tm, __shfl_xor(tm, 32));
    const float mnew = fmaxf(mrow, tm);
    const float alpha = __builtin_amdgcn_exp2f((mrow - mnew) * L2E);
    mrow = mnew;
    float p[4], ps = 0.f;
#pragma unroll
    for (int j = 0; j < 4; ++j) { p[j] = __builtin_amdgcn_exp2f((sv[j] - mnew) * L2E); ps += p[j]; }
    lrow = lrow * alpha + ps;
#pragma unroll
    for (int i = 0; i < 8; ++i) o[i] *= alpha;
    const u32x2 pf = {cvtpk(p[0], p[1]), cvtpk(p[2], p[3])};
#pragma unroll
    for (int i = 0; i < 8; ++i) {
      const int w = i >> 1; const unsigned sel = (i & 1) ? 0x07060302u : 0x05040100u;
      const u32x2 vt = {__builtin_amdgcn_perm(vf[st][1][w], vf[st][0][w], sel), __builtin_amdgcn_perm(vf[st][3][w], vf[st][2][w], sel)};
      o[i] = __builtin_amdgcn_mfma_f32_16x16x16bf16_1k(__builtin_bit_cast(bf16x4, vt), __builtin_bit_cast(bf16x4, pf), o[i], 0, 0, 0);
    }
    if (t + DEPTH < NT) { issue(t + DEPTH, st); PIN(); }
    if (t == NT - 3) {
      const uint16_t* wp = wo + (size_t)(hkv * 512 + wave * 64 + 8 * g) * 512 + s * 128 + 8 * m;
#pragma unroll
      for (int ks = 0; ks < 2; ++ks)
#pragma unroll
        for (int j = 0; j < 8; ++j) wr[ks][j] = ld(wp + (size_t)(ks * 32 + j) * 512);
      PIN();
    }
  }
  STAMP(2);
  lrow += __shfl_xor(lrow, 16);
  lrow += __shfl_xor(lrow, 32);
  if (g == 0) { sm[wave][m] = mrow; sl[wave][m] = lrow; }
#pragma unroll
  for (int r = 0; r < 4; ++r) {
    f32x4 a = {o[0][r], o[1][r], o[2][r], o[3][r]};
    f32x4 c = {o[4][r], o[5][r], o[6][r], o[7][r]};
    *(f32x4*)&so[wave][m][32 * g + 8 * r] = a;
    *(f32x4*)&so[wave][m][32 * g + 8 * r + 4] = c;
  }
  __syncthreads();
  const int p0 = (b * HKV + hkv) * NSPLIT, pidx = p0 + s;
  const int row = tid >> 5, d0 = (tid & 31) * 4;
  const __amdgpu_buffer_rsrc_t rsO = __builtin_amdgcn_make_buffer_rsrc((void*)part_o, 0, 256 * 2048 * 4, 0x00020000);
  f32x4 po[NSPLIT];
  float pm[NSPLIT], pl[NSPLIT];
  {
    float Mx = sm[0][row];
#pragma unroll
    for (int w = 1; w < NW; ++w) Mx = fmaxf(Mx, sm[w][row]);
    f32x4 a = {0.f, 0.f, 0.f, 0.f};
    float L = 0.f;
#pragma unroll
    for (int w = 0; w < NW; ++w) { const float ww = __builtin_amdgcn_exp2f((sm[w][row] - Mx) * L2E); a += *(const f32x4*)&so[w][row][d0] * ww; L += sl[w][row] * ww; }
#pragma unroll
    for (int q = 0; q < NSPLIT; ++q) { po[q] = a; pm[q] = Mx; pl[q] = L; }
    __builtin_amdgcn_raw_buffer_store_b128(__builtin_bit_cast(u32x4, a), rsO, (pidx * 2048 + row * 128 + d0) * 4, 0, AUX_SC1);
    if ((tid & 31) == 0)
      __hip_atomic_store((gu64*)(part_ml + (pidx * 16 + row) * 2), ((unsigned long long)__float_as_uint(L) << 32) | __float_as_uint(Mx), RLX_AGENT);
  }
  DRAIN();
  __syncthreads();
  gu32* flagA = (gu32*)(sync + (b * HKV + hkv) * 16);
  if (tid == 0) {
    __hip_atomic_store(flagA + s, 1u, RLX_AGENT);
    const __amdgpu_buffer_rsrc_t rsF = __builtin_amdgcn_make_buffer_rsrc((void*)sync, 0, 64 * 64, 0x00020000);
    for (unsigned spins = 0; spins < (1u << 22); ++spins) {
      const u32x4 f = __builtin_amdgcn_raw_buffer_load_b128(rsF, (b * HKV + hkv) * 64, 0, AUX_SC1);
      if ((f[0] & f[1] & f[2] & f[3]) == 1u) break;
      __builtin_amdgcn_s_sleep(1);
    }
  }
  __syncthreads();
  STAMP(3);
#pragma unroll
  for (int q = 0; q < NSPLIT; ++q) {
    if (q != s) {
      po[q] = __builtin_bit_cast(f32x4, __builtin_amdgcn_raw_buffer_load_b128(rsO, ((p0 + q) * 2048 + row * 128 + d0) * 4, 0, AUX_SC1));
      const unsigned long long ml = __hip_atomic_load((gu64*)(part_ml + ((p0 + q) * 16 + row) * 2), RLX_AGENT);
      pm[q] = __uint_as_float((unsigned)ml); pl[q] = __uint_as_float((unsigned)(ml >> 32));
    }
  }
  {
    float Mx = pm[0];
#pragma unroll
    for (int q = 1; q < NSPLIT; ++q) Mx = fmaxf(Mx, pm[q]);
    float L = 0.f;
    f32x4 a = {0.f, 0.f, 0.f, 0.f};
#pragma unroll
    for (int q = 0; q < NSPLIT; ++q) { const float w = __builtin_amdgcn_exp2f((pm[q] - Mx) * L2E); L += pl[q] * w; a += po[q] * w; }
    const float inv = 1.f / L;
    u32x2 pk = {cvtpk(a[0] * inv, a[1] * inv), cvtpk(a[2] * inv, a[3] * inv)};
    *(u32x2*)&qs[row & 3][(row >> 2) * 128 + d0] = pk;
  }
  __syncthreads();
  {
    f32x4 acc[8];
#pragma unroll
    for (int i = 0; i < 8; ++i) acc[i] = (f32x4){0.f, 0.f, 0.f, 0.f};
#pragma unroll
    for (int ks = 0; ks < 2; ++ks) {
      const u32x4 af = *(const u32x4*)&qs[m & 3][wave * 64 + ks * 32 + 8 * g];
#pragma unroll
      for (int i = 0; i < 8; ++i) {
        u32x4 c = tcol(wr[ks], i);
        acc[i] = __builtin_amdgcn_mfma_f32_16x16x32_bf16(asbf(af), asbf(c), acc[i], 0, 0, 0);
      }
    }
    float* red = &so[0][0][0];
    if (g == 0) {
#pragma unroll
      for (int r = 0; r < 4; ++r) {
        f32x4 a = {acc[0][r], acc[1][r], acc[2][r], acc[3][r]};
        f32x4 c = {acc[4][r], acc[5][r], acc[6][r], acc[7][r]};
        *(f32x4*)&red[(wave * 4 + r) * 132 + 8 * m] = a;
        *(f32x4*)&red[(wave * 4 + r) * 132 + 8 * m + 4] = c;
      }
    }
  }
  __syncthreads();
  gu64* gran = (gu64*)outp + (size_t)(b * 4 + s) * 8 * 512 + tid;
  {
    const float* red = &so[0][0][0];
    const int r = tid >> 7, c = tid & 127;
    float a = red[r * 132 + c];
#pragma unroll
    for (int w = 1; w < NW; ++w) a += red[(w * 4 + r) * 132 + c];
    __hip_atomic_store(gran + hkv * 512, (1ull << 32) | __float_as_uint(a), RLX_AGENT);
  }
  if (hkv == 0) {
    float v[HKV];
    for (unsigned spins = 0; spins < (1u << 20); ++spins) {
      bool ok = true;
#pragma unroll
      for (int h = 0; h < HKV; ++h) { const unsigned long long x = __hip_atomic_load(gran + h * 512, RLX_AGENT); v[h] = __uint_as_float((unsigned)x); ok &= (x >> 32) == 1ull; }
      if (__all(ok)) break;
      __builtin_amdgcn_s_sleep(1);
    }
    float a = v[0];
#pragma unroll
    for (int h = 1; h < HKV; ++h) a += v[h];
    const unsigned pk = cvtpk(a, a);
    out[(b * 4 + (tid >> 7)) * 512 + s * 128 + (tid & 127)] = (uint16_t)(pk & 0xffffu);
  }
  DRAIN();
  STAMP(4);
}

extern "C" void kernel_launch(void* const* d_in, const int* in_sizes, int n_in, void* d_out, int out_size, void* d_ws, size_t ws_size, hipStream_t stream) {
  const uint16_t* x = (const uint16_t*)d_in[0];
  const uint16_t* kc = (const uint16_t*)d_in[1];
  const uint16_t* vc = (const uint16_t*)d_in[2];
  const uint16_t* wq = (const uint16_t*)d_in[3];
  const uint16_t* wo = (const uint16_t*)d_in[4];
  char* ws = (char*)d_ws;
  float* qpart = (float*)(ws + WS_QPART);
  float* part_o = (float*)(ws + WS_PARTO);
  float* part_ml = (float*)(ws + WS_PARTML);
  float* outp = (float*)(ws + WS_OUTP);
  unsigned* sync = (unsigned*)(ws + WS_SYNC);
  qproj_kernel<<<256, 256, 0, stream>>>(x, wq, qpart, (u32x4*)(ws + WS_OUTP), kc DBGPASS);
  attn_kernel<<<256, 512, 0, stream>>>(qpart, kc, vc, wo, part_o, part_ml, outp, sync, (uint16_t*)d_out DBGPASS);
}
```

```cpp
#include <hip/hip_runtime.h>
#include <hip/hip_bf16.h>
#include <stdint.h>

typedef __attribute__((ext_vector_type(8))) short bf16x8;
typedef __attribute__((ext_vector_type(4))) short bf16x4;
typedef __attribute__((ext_vector_type(4))) float f32x4;
typedef __attribute__((ext_vector_type(4))) unsigned u32x4;
typedef __attribute__((ext_vector_type(2))) unsigned u32x2;
typedef __attribute__((ext_vector_type(2))) __bf16 bf16x2_t;
typedef __attribute__((ext_vector_type(2))) float f32x2_t;
#define GAS __attribute__((address_space(1)))
typedef GAS unsigned gu32;
typedef GAS unsigned long long gu64;
#define RLX_AGENT __ATOMIC_RELAXED, __HIP_MEMORY_SCOPE_AGENT
#define PIN() do { asm volatile("" ::: "memory"); __builtin_amdgcn_sched_barrier(0); } while (0)
#define DRAIN() asm volatile("s_waitcnt vmcnt(0)" ::: "memory")

constexpr int B = 8, SQ = 4, DM = 512, HQ = 32, HKV = 8, SK = 4096, DH = 128;
constexpr int NSPLIT = 4, NW = 8, KPW = SK / NSPLIT / NW, TILE = 16, NT = KPW / TILE;
#define DEPTH 2
constexpr int QP = 4;
constexpr float L2E = 1.4426950408889634f;
constexpr float SCALE = 0.08838834764831845f;
constexpr int AUX_SC1 = 16;

constexpr size_t WS_QPART = 0;
constexpr size_t WS_PARTO = 2u << 20;
constexpr size_t WS_OUTP = 4u << 20;
constexpr size_t WS_PARTML = 5u << 20;
constexpr size_t WS_SYNC = WS_PARTML + (32u << 10);
constexpr int ZERO_BYTES = (1 << 20) + (32 << 10) + 64 * 64;

__device__ __forceinline__ unsigned cvtpk(float lo, float hi) { f32x2_t v = {lo, hi}; bf16x2_t r = __builtin_convertvector(v, bf16x2_t); return __builtin_bit_cast(unsigned, r); }
__device__ __forceinline__ float bf16r(float x) { unsigned u = cvtpk(x, x); return __uint_as_float(u << 16); }
__device__ __forceinline__ u32x4 ldnt(const void* p) { return __builtin_nontemporal_load((const u32x4*)p); }
__device__ __forceinline__ u32x4 ld(const void* p) { return *(const u32x4*)p; }
__device__ __forceinline__ bf16x8 asbf(u32x4 v) { return __builtin_bit_cast(bf16x8, v); }
__device__ __forceinline__ u32x4 tcol(const u32x4* R, int i) {
  const int w = i >> 1; const unsigned sel = (i & 1) ? 0x07060302u : 0x05040100u;
  u32x4 o;
  o[0] = __builtin_amdgcn_perm(R[1][w], R[0][w], sel);
  o[1] = __builtin_amdgcn_perm(R[3][w], R[2][w], sel);
  o[2] = __builtin_amdgcn_perm(R[5][w], R[4][w], sel);
  o[3] = __builtin_amdgcn_perm(R[7][w], R[6][w], sel);
  return o;
}

__global__ __launch_bounds__(256) void qproj_kernel(const uint16_t* __restrict__ x, const uint16_t* __restrict__ wq, float* __restrict__ qpart, u32x4* __restrict__ zero) {
  __shared__ float red[4][32][132];
  if (blockIdx.x >= 128) {
    const int p = blockIdx.x - 128;
    for (int i = p * 256 + threadIdx.x; i < ZERO_BYTES / 16; i += 128 * 256) zero[i] = (u32x4){0u, 0u, 0u, 0u};
    return;
  }
  const int cc = blockIdx.x & 31, kq = blockIdx.x >> 5;
  const int wave = threadIdx.x >> 6, lane = threadIdx.x & 63, g = lane >> 4, m = lane & 15;
  const int kb = kq * 128 + wave * 32;
  u32x4 R[8], A[2];
#pragma unroll
  for (int j = 0; j < 8; ++j) R[j] = ld(wq + (size_t)(kb + 8 * g + j) * 4096 + cc * 128 + 8 * m);
#pragma unroll
  for (int rt = 0; rt < 2; ++rt) A[rt] = ld(x + (rt * 16 + m) * 512 + kb + 8 * g);
  f32x4 acc[2][8];
#pragma unroll
  for (int i = 0; i < 8; ++i) {
    u32x4 c = tcol(R, i);
#pragma unroll
    for (int rt = 0; rt < 2; ++rt)
      acc[rt][i] = __builtin_amdgcn_mfma_f32_16x16x32_bf16(asbf(A[rt]), asbf(c), (f32x4){0.f, 0.f, 0.f, 0.f}, 0, 0, 0);
  }
#pragma unroll
  for (int rt = 0; rt < 2; ++rt)
#pragma unroll
    for (int r = 0; r < 4; ++r)
#pragma unroll
      for (int i = 0; i < 8; ++i) red[wave][rt * 16 + 4 * g + r][8 * m + i] = acc[rt][i][r];
  __syncthreads();
  const int t = threadIdx.x, row = t >> 3, c0 = (t & 7) * 16;
  float* dst = qpart + ((size_t)kq * 32 + row) * 4096 + cc * 128 + c0;
#pragma unroll
  for (int e = 0; e < 4; ++e) {
    f32x4 s = *(const f32x4*)&red[0][row][c0 + 4 * e];
#pragma unroll
    for (int w = 1; w < 4; ++w) s += *(const f32x4*)&red[w][row][c0 + 4 * e];
    *(f32x4*)(dst + 4 * e) = s;
  }
}

__global__ __launch_bounds__(512, 2) void attn_kernel(const float* __restrict__ qpart, const uint16_t* __restrict__ kc, const uint16_t* __restrict__ vc,
                                                      const uint16_t* __restrict__ wo, float* part_o, float* part_ml, float* outp, unsigned* sync,
                                                      uint16_t* __restrict__ out) {
  __shared__ float so[NW][16][132];
  __shared__ float sm[NW][16], sl[NW][16];
  __shared__ uint16_t qs[4][520];
  const int wg = blockIdx.x, hkv = wg & 7, ii = wg >> 3, b = ii >> 2, s = ii & 3;
  const int tid = threadIdx.x, wave = tid >> 6, lane = tid & 63, g = lane >> 4, m = lane & 15;
  const size_t kvbase = ((size_t)(b * HKV + hkv) * SK + s * (SK / NSPLIT) + wave * TILE) * DH;
  const uint16_t* kp = kc + kvbase + m * DH + 8 * g;
  const uint16_t* vp = vc + kvbase + (4 * g) * DH + 8 * m;
  f32x4 qa[QP];
  {
    const float* qp = qpart + (size_t)(b * 4 + (tid >> 7)) * 4096 + hkv * 512 + (tid & 127) * 4;
#pragma unroll
    for (int p = 0; p < QP; ++p) qa[p] = *(const f32x4*)(qp + (size_t)p * 32 * 4096);
  }
  PIN();
  __builtin_amdgcn_s_barrier();
  PIN();
  u32x4 kf[DEPTH][4], vf[DEPTH][4];
  auto issue = [&](int t, int st) {
#pragma unroll
    for (int c = 0; c < 4; ++c) kf[st][c] = ldnt(kp + (size_t)(t * NW * TILE) * DH + 32 * c);
#pragma unroll
    for (int j = 0; j < 4; ++j) vf[st][j] = ldnt(vp + (size_t)(t * NW * TILE + j) * DH);
  };
#pragma unroll
  for (int d = 0; d < DEPTH; ++d) issue(d, d);
  PIN();
  {
    f32x4 qsum = qa[0];
#pragma unroll
    for (int p = 1; p < QP; ++p) qsum += qa[p];
    u32x2 pk = {cvtpk(qsum[0], qsum[1]), cvtpk(qsum[2], qsum[3])};
    *(u32x2*)&qs[tid >> 7][(tid & 127) * 4] = pk;
  }
  __syncthreads();
  u32x4 qf[4];
#pragma unroll
  for (int c = 0; c < 4; ++c) qf[c] = *(const u32x4*)&qs[m & 3][(m >> 2) * 128 + 32 * c + 8 * g];
  f32x4 o[8];
#pragma unroll
  for (int i = 0; i < 8; ++i) o[i] = (f32x4){0.f, 0.f, 0.f, 0.f};
  float mrow = -__builtin_inff(), lrow = 0.f;
  u32x4 wr[2][8];
#pragma unroll
  for (int t = 0; t < NT; ++t) {
    const int st = t % DEPTH;
    f32x4 sc = {0.f, 0.f, 0.f, 0.f};
#pragma unroll
    for (int c = 0; c < 4; ++c) sc = __builtin_amdgcn_mfma_f32_16x16x32_bf16(asbf(kf[st][c]), asbf(qf[c]), sc, 0, 0, 0);
    float sv[4];
#pragma unroll
    for (int j = 0; j < 4; ++j) sv[j] = bf16r(bf16r(sc[j]) * SCALE);
    float tm = fmaxf(fmaxf(sv[0], sv[1]), fmaxf(sv[2], sv[3]));
    tm = fmaxf(tm, __shfl_xor(tm, 16));
    tm = fmaxf(tm, __shfl_xor(tm, 32));
    const float mnew = fmaxf(mrow, tm);
    const float alpha = __builtin_amdgcn_exp2f((mrow - mnew) * L2E);
    mrow = mnew;
    float p[4], ps = 0.f;
#pragma unroll
    for (int j = 0; j < 4; ++j) { p[j] = __builtin_amdgcn_exp2f((sv[j] - mnew) * L2E); ps += p[j]; }
    lrow = lrow * alpha + ps;
#pragma unroll
    for (int i = 0; i < 8; ++i) o[i] *= alpha;
    const u32x2 pf = {cvtpk(p[0], p[1]), cvtpk(p[2], p[3])};
#pragma unroll
    for (int i = 0; i < 8; ++i) {
      const int w = i >> 1; const unsigned sel = (i & 1) ? 0x07060302u : 0x05040100u;
      const u32x2 vt = {__builtin_amdgcn_perm(vf[st][1][w], vf[st][0][w], sel), __builtin_amdgcn_perm(vf[st][3][w], vf[st][2][w], sel)};
      o[i] = __builtin_amdgcn_mfma_f32_16x16x16bf16_1k(__builtin_bit_cast(bf16x4, vt), __builtin_bit_cast(bf16x4, pf), o[i], 0, 0, 0);
    }
    if (t + DEPTH < NT) { issue(t + DEPTH, st); PIN(); }
    if (t == NT - 3) {
      const uint16_t* wp = wo + (size_t)(hkv * 512 + wave * 64 + 8 * g) * 512 + s * 128 + 8 * m;
#pragma unroll
      for (int ks = 0; ks < 2; ++ks)
#pragma unroll
        for (int j = 0; j < 8; ++j) wr[ks][j] = ld(wp + (size_t)(ks * 32 + j) * 512);
      PIN();
    }
  }
  lrow += __shfl_xor(lrow, 16);
  lrow += __shfl_xor(lrow, 32);
  if (g == 0) { sm[wave][m] = mrow; sl[wave][m] = lrow; }
#pragma unroll
  for (int r = 0; r < 4; ++r) {
    f32x4 a = {o[0][r], o[1][r], o[2][r], o[3][r]};
    f32x4 c = {o[4][r], o[5][r], o[6][r], o[7][r]};
    *(f32x4*)&so[wave][m][32 * g + 8 * r] = a;
    *(f32x4*)&so[wave][m][32 * g + 8 * r + 4] = c;
  }
  __syncthreads();
  const int p0 = (b * HKV + hkv) * NSPLIT, pidx = p0 + s;
  const int row = tid >> 5, d0 = (tid & 31) * 4;
  const __amdgpu_buffer_rsrc_t rsO = __builtin_amdgcn_make_buffer_rsrc((void*)part_o, 0, 256 * 2048 * 4, 0x00020000);
  f32x4 po[NSPLIT];
  float pm[NSPLIT], pl[NSPLIT];
  {
    float Mx = sm[0][row];
#pragma unroll
    for (int w = 1; w < NW; ++w) Mx = fmaxf(Mx, sm[w][row]);
    f32x4 a = {0.f, 0.f, 0.f, 0.f};
    float L = 0.f;
#pragma unroll
    for (int w = 0; w < NW; ++w) { const float ww = __builtin_amdgcn_exp2f((sm[w][row] - Mx) * L2E); a += *(const f32x4*)&so[w][row][d0] * ww; L += sl[w][row] * ww; }
#pragma unroll
    for (int q = 0; q < NSPLIT; ++q) { po[q] = a; pm[q] = Mx; pl[q] = L; }
    __builtin_amdgcn_raw_buffer_store_b128(__builtin_bit_cast(u32x4, a), rsO, (pidx * 2048 + row * 128 + d0) * 4, 0, AUX_SC1);
    if ((tid & 31) == 0)
      __hip_atomic_store((gu64*)(part_ml + (pidx * 16 + row) * 2), ((unsigned long long)__float_as_uint(L) << 32) | __float_as_uint(Mx), RLX_AGENT);
  }
  DRAIN();
  __syncthreads();
  gu32* flagA = (gu32*)(sync + (b * HKV + hkv) * 16);
  if (tid == 0) {
    __hip_atomic_store(flagA + s, 1u, RLX_AGENT);
    const __amdgpu_buffer_rsrc_t rsF = __builtin_amdgcn_make_buffer_rsrc((void*)sync, 0, 64 * 64, 0x00020000);
    for (unsigned spins = 0; spins < (1u << 16); ++spins) {
      const u32x4 f = __builtin_amdgcn_raw_buffer_load_b128(rsF, (b * HKV + hkv) * 64, 0, AUX_SC1);
      if ((f[0] & f[1] & f[2] & f[3]) == 1u) break;
      __builtin_amdgcn_s_sleep(1);
    }
  }
  __syncthreads();
#pragma unroll
  for (int q = 0; q < NSPLIT; ++q) {
    if (q != s) {
      po[q] = __builtin_bit_cast(f32x4, __builtin_amdgcn_raw_buffer_load_b128(rsO, ((p0 + q) * 2048 + row * 128 + d0) * 4, 0, AUX_SC1));
      const unsigned long long ml = __hip_atomic_load((gu64*)(part_ml + ((p0 + q) * 16 + row) * 2), RLX_AGENT);
      pm[q] = __uint_as_float((unsigned)ml); pl[q] = __uint_as_float((unsigned)(ml >> 32));
    }
  }
  {
    float Mx = pm[0];
#pragma unroll
    for (int q = 1; q < NSPLIT; ++q) Mx = fmaxf(Mx, pm[q]);
    float L = 0.f;
    f32x4 a = {0.f, 0.f, 0.f, 0.f};
#pragma unroll
    for (int q = 0; q < NSPLIT; ++q) { const float w = __builtin_amdgcn_exp2f((pm[q] - Mx) * L2E); L += pl[q] * w; a += po[q] * w; }
    const float inv = 1.f / L;
    u32x2 pk = {cvtpk(a[0] * inv, a[1] * inv), cvtpk(a[2] * inv, a[3] * inv)};
    *(u32x2*)&qs[row & 3][(row >> 2) * 128 + d0] = pk;
  }
  __syncthreads();
  {
    f32x4 acc[8];
#pragma unroll
    for (int i = 0; i < 8; ++i) acc[i] = (f32x4){0.f, 0.f, 0.f, 0.f};
#pragma unroll
    for (int ks = 0; ks < 2; ++ks) {
      const u32x4 af = *(const u32x4*)&qs[m & 3][wave * 64 + ks * 32 + 8 * g];
#pragma unroll
      for (int i = 0; i < 8; ++i) {
        u32x4 c = tcol(wr[ks], i);
        acc[i] = __builtin_amdgcn_mfma_f32_16x16x32_bf16(asbf(af), asbf(c), acc[i], 0, 0, 0);
      }
    }
    float* red = &so[0][0][0];
    if (g == 0) {
#pragma unroll
      for (int r = 0; r < 4; ++r) {
        f32x4 a = {acc[0][r], acc[1][r], acc[2][r], acc[3][r]};
        f32x4 c = {acc[4][r], acc[5][r], acc[6][r], acc[7][r]};
        *(f32x4*)&red[(wave * 4 + r) * 132 + 8 * m] = a;
        *(f32x4*)&red[(wave * 4 + r) * 132 + 8 * m + 4] = c;
      }
    }
  }
  __syncthreads();
  gu64* gran = (gu64*)outp + (size_t)(b * 4 + s) * 8 * 512 + tid;
  {
    const float* red = &so[0][0][0];
    const int r = tid >> 7, c = tid & 127;
    float a = red[r * 132 + c];
#pragma unroll
    for (int w = 1; w < NW; ++w) a += red[(w * 4 + r) * 132 + c];
    __hip_atomic_store(gran + hkv * 512, (1ull << 32) | __float_as_uint(a), RLX_AGENT);
  }
  if (hkv == 0) {
    float v[HKV];
    for (unsigned spins = 0; spins < (1u << 16); ++spins) {
      bool ok = true;
#pragma unroll
      for (int h = 0; h < HKV; ++h) { const unsigned long long x = __hip_atomic_load(gran + h * 512, RLX_AGENT); v[h] = __uint_as_float((unsigned)x); ok &= (x >> 32) == 1ull; }
      if (__all(ok)) break;
      __builtin_amdgcn_s_sleep(1);
    }
    float a = v[0];
#pragma unroll
    for (int h = 1; h < HKV; ++h) a += v[h];
    const unsigned pk = cvtpk(a, a);
    out[(b * 4 + (tid >> 7)) * 512 + s * 128 + (tid & 127)] = (uint16_t)(pk & 0xffffu);
  }
}

extern "C" void kernel_launch(void* const* d_in, const int* in_sizes, int n_in, void* d_out, int out_size, void* d_ws, size_t ws_size, hipStream_t stream) {
  const uint16_t* x = (const uint16_t*)d_in[0];
  const uint16_t* kc = (const uint16_t*)d_in[1];
  const uint16_t* vc = (const uint16_t*)d_in[2];
  const uint16_t* wq = (const uint16_t*)d_in[3];
  const uint16_t* wo = (const uint16_t*)d_in[4];
  char* ws = (char*)d_ws;
  float* qpart = (float*)(ws + WS_QPART);
  float* part_o = (float*)(ws + WS_PARTO);
  float* part_ml = (float*)(ws + WS_PARTML);
  float* outp = (float*)(ws + WS_OUTP);
  unsigned* sync = (unsigned*)(ws + WS_SYNC);
  qproj_kernel<<<256, 256, 0, stream>>>(x, wq, qpart, (u32x4*)(ws + WS_OUTP));
  attn_kernel<<<256, 512, 0, stream>>>(qpart, kc, vc, wo, part_o, part_ml, outp, sync, (uint16_t*)d_out);
}
```
